```python
import torch

def _softmax(x, axis=-1):
    return torch.softmax(x, dim=axis)
def _gelu(x):
    return torch.nn.functional.gelu(x, approximate="tanh")
def _silu(x):
    return torch.nn.functional.silu(x)
def _sigmoid(x):
    return torch.sigmoid(x)
def _layernorm(x, axis=-1, eps=1e-5):
    m = x.mean(dim=axis, keepdim=True)
    v = x.var(dim=axis, keepdim=True, unbiased=False)
    return (x - m) / torch.sqrt(v + eps)
def _scatter_add(tgt, idx, src, axis=0):
    return tgt.clone().index_add_(axis, idx, src)
def _segment_sum(src, seg, n):
    out = torch.zeros((n,) + src.shape[1:], dtype=src.dtype)
    return out.index_add_(0, seg, src)
def _pad(x, pw):
    flat = tuple(v for pair in reversed(pw) for v in pair)
    return torch.nn.functional.pad(x, flat)

kernel_name = 'concat_heads__mha_block_cached__mha_block_cached__near_ridge'

def setup_inputs():

    _w = (lambda a: (lambda t: t.to(torch.bfloat16) if t.is_floating_point() else t)(torch.as_tensor(a)))
    def _A_setup():
        B, Sq, D = 16, 96, 576
        H, Sk, Dh = 32, 512, 128
        return {
            "x": _w((lambda *s: torch.randn(*s, dtype=torch.bfloat16) * 0.8)(B, Sq, D)),
            "k_cache": _w((lambda *s: torch.randn(*s, dtype=torch.bfloat16) * 0.8)(B, H, Sk, Dh)),
            "v_cache": _w((lambda *s: torch.randn(*s, dtype=torch.bfloat16) * 0.05)(B, H, Sk, Dh)),
            "wq": _w((lambda *s: torch.randn(*s, dtype=torch.bfloat16) * 0.8)(D, H * Dh) * (D ** -0.5)),
            "wo": _w((lambda *s: torch.randn(*s, dtype=torch.bfloat16) * 0.05)(H * Dh, D) * ((H * Dh) ** -0.5)),
        }
    _A_in = _A_setup()

    def _B_setup():
        B, Sq, D = 16, 96, 576
        H, Sk, Dh = 32, 512, 128
        return {
            "x": _w((lambda *s: torch.randn(*s, dtype=torch.bfloat16) * 0.8)(B, Sq, D)),
            "k_cache": _w((lambda *s: torch.randn(*s, dtype=torch.bfloat16) * 0.8)(B, H, Sk, Dh)),
            "v_cache": _w((lambda *s: torch.randn(*s, dtype=torch.bfloat16) * 0.05)(B, H, Sk, Dh)),
            "wq": _w((lambda *s: torch.randn(*s, dtype=torch.bfloat16) * 0.8)(D, H * Dh) * (D ** -0.5)),
            "wo": _w((lambda *s: torch.randn(*s, dtype=torch.bfloat16) * 0.05)(H * Dh, D) * ((H * Dh) ** -0.5)),
        }
    _B_in = _B_setup()

    return {
        "A_x": _A_in["x"],
        "A_k_cache": _A_in["k_cache"],
        "A_v_cache": _A_in["v_cache"],
        "A_wq": _A_in["wq"],
        "A_wo": _A_in["wo"],
        "B_x": _B_in["x"],
        "B_k_cache": _B_in["k_cache"],
        "B_v_cache": _B_in["v_cache"],
        "B_wq": _B_in["wq"],
        "B_wo": _B_in["wo"],
    }

def reference(A_x, A_k_cache, A_v_cache, A_wq, A_wo, B_x, B_k_cache, B_v_cache, B_wq, B_wo):
    def _A_ref(x, k_cache, v_cache, wq, wo):
        B, Sq, D = x.shape
        H, Dh = 32, 128
        q = (lambda x, *d: x.permute(*d))((x @ wq).reshape(B, Sq, H, Dh), 0, 2, 1, 3)
        scores = torch.einsum("bhqd,bhkd->bhqk", q, k_cache) * (Dh ** -0.5)
        p = _softmax(scores, axis=-1)
        o = torch.einsum("bhqk,bhkd->bhqd", p, v_cache)
        return (lambda x, *d: x.permute(*d))(o, 0, 2, 1, 3).reshape(B, Sq, H * Dh) @ wo

    def _B_ref(x, k_cache, v_cache, wq, wo):
        B, Sq, D = x.shape
        H, Dh = 32, 128
        q = (lambda x, *d: x.permute(*d))((x @ wq).reshape(B, Sq, H, Dh), 0, 2, 1, 3)
        scores = torch.einsum("bhqd,bhkd->bhqk", q, k_cache) * (Dh ** -0.5)
        p = _softmax(scores, axis=-1)
        o = torch.einsum("bhqk,bhkd->bhqd", p, v_cache)
        return (lambda x, *d: x.permute(*d))(o, 0, 2, 1, 3).reshape(B, Sq, H * Dh) @ wo

    a = _A_ref(A_x, A_k_cache, A_v_cache, A_wq, A_wo)
    b = _B_ref(B_x, B_k_cache, B_v_cache, B_wq, B_wo)
    return torch.concatenate([a, b], axis=-1)
```

```cpp
#include <hip/hip_runtime.h>
#include <hip/hip_bf16.h>
#include <stdint.h>

typedef __attribute__((ext_vector_type(8))) __bf16 bf16x8;
typedef __attribute__((ext_vector_type(4))) __bf16 bf16x4;
typedef __attribute__((ext_vector_type(2))) __bf16 bf16x2;
typedef __attribute__((ext_vector_type(4))) short s16x4;
typedef __attribute__((ext_vector_type(4))) float f32x4;
typedef __attribute__((ext_vector_type(2))) float f32x2;
typedef __attribute__((ext_vector_type(4))) unsigned u32x4;
typedef __attribute__((ext_vector_type(2))) unsigned u32x2;
#define LAS __attribute__((address_space(3)))

constexpr int NB = 16, SQ = 96, DM = 576, NH = 32, SK = 512, DH = 128;
constexpr float QSCALE = 0.08838834764831845f * 1.4426950408889634f;

#define MFMA(a, b, c) __builtin_amdgcn_mfma_f32_16x16x32_bf16(a, b, c, 0, 0, 0)
#define WAIT_BAR(N) asm volatile("s_waitcnt vmcnt(" #N ") lgkmcnt(0)\n\ts_barrier" ::: "memory")

__device__ __forceinline__ void dma(const char* sbase, unsigned voff, unsigned ldsbase) {
  asm volatile("s_mov_b32 m0, %2\n\ts_nop 0\n\tglobal_load_lds_dwordx4 %0, %1" ::"v"(voff), "s"(sbase), "s"(ldsbase) : "memory");
}
__device__ __forceinline__ void dma4_nt(const char* sbase, unsigned v0, unsigned v1, unsigned v2, unsigned v3, unsigned ldsbase) {
  asm volatile("s_mov_b32 m0, %5\n\ts_nop 0\n\t"
               "global_load_lds_dwordx4 %0, %4 nt\n\t"
               "global_load_lds_dwordx4 %1, %4 offset:1024 nt\n\t"
               "global_load_lds_dwordx4 %2, %4 offset:2048 nt\n\t"
               "global_load_lds_dwordx4 %3, %4 offset:3072 nt"
               ::"v"(v0), "v"(v1), "v"(v2), "v"(v3), "s"(sbase), "s"(ldsbase) : "memory");
}
__device__ __forceinline__ s16x4 tr_read(unsigned a) { return __builtin_amdgcn_ds_read_tr16_b64_v4i16((LAS s16x4*)a); }
__device__ __forceinline__ bf16x8 cat8(s16x4 lo, s16x4 hi) {
  return __builtin_bit_cast(bf16x8, __builtin_shufflevector(lo, hi, 0, 1, 2, 3, 4, 5, 6, 7));
}
__device__ __forceinline__ unsigned pk2(float a, float b) {
  f32x2 v = {a, b};
  return __builtin_bit_cast(unsigned, __builtin_convertvector(v, bf16x2));
}
__device__ __forceinline__ bf16x8 pack8(f32x4 a, f32x4 b) {
  u32x4 w = {pk2(a[0], a[1]), pk2(a[2], a[3]), pk2(b[0], b[1]), pk2(b[2], b[3])};
  return __builtin_bit_cast(bf16x8, w);
}
__device__ __forceinline__ float gmax(float x) {
  auto a = __builtin_amdgcn_permlane16_swap(__float_as_uint(x), __float_as_uint(x), false, false);
  x = fmaxf(__uint_as_float(a[0]), __uint_as_float(a[1]));
  auto b = __builtin_amdgcn_permlane32_swap(__float_as_uint(x), __float_as_uint(x), false, false);
  return fmaxf(__uint_as_float(b[0]), __uint_as_float(b[1]));
}
__device__ __forceinline__ float gsum(float x) {
  auto a = __builtin_amdgcn_permlane16_swap(__float_as_uint(x), __float_as_uint(x), false, false);
  x = __uint_as_float(a[0]) + __uint_as_float(a[1]);
  auto b = __builtin_amdgcn_permlane32_swap(__float_as_uint(x), __float_as_uint(x), false, false);
  return __uint_as_float(b[0]) + __uint_as_float(b[1]);
}

constexpr int SLOT = 16384;
constexpr int XOFF = 3 * SLOT;
constexpr int XSLOT = 12288;
constexpr int LDS_A = 5 * SLOT;

constexpr size_t OF_BRANCH = (size_t)NH * 4 * 96 * 1024;

__global__ __launch_bounds__(256, 2) void attn_kernel(const char* xA, const char* kA, const char* vA, const char* wqA,
                                                     const char* xB, const char* kB, const char* vB, const char* wqB, char* of) {
  extern __shared__ __attribute__((aligned(1024))) char lds[];
  const unsigned lds0 = (unsigned)(uintptr_t)lds;
  const int lane = threadIdx.x & 63, w = __builtin_amdgcn_readfirstlane(threadIdx.x >> 6);
  const int id = blockIdx.x, xcd = id & 7, r = id >> 3;
  const int br = xcd >> 2, late = r >> 5, b = (r & 31) >> 1, sub = r & 1;
  const int hj = w >> 1, rh = w & 1;
  const int head = (xcd & 3) * 8 + sub * 4 + late * 2 + hj;
  const char* xg = (br ? xB : xA) + (size_t)b * SQ * DM * 2;
  const char* wq = (br ? wqB : wqA) + head * 256;
  const char* kg = (br ? kB : kA) + ((size_t)b * NH + head) * SK * DH * 2 + rh * 4096;
  const char* vg = (br ? vB : vA) + ((size_t)b * NH + head) * SK * DH * 2 + rh * 4096;
  const int g = lane >> 4, c16 = lane & 15;

  const unsigned wv0 = g * 8192 + ((c16 ^ (g << 1)) << 4), wv1 = g * 8192 + ((c16 ^ ((g | 4) << 1)) << 4);
  const unsigned xv0 = (lane >> 3) * 1152 + (((lane & 7) ^ g) << 4), xv1 = (lane >> 3) * 1152 + (((lane & 7) ^ (g | 4)) << 4);
  const unsigned kv0 = g * 256 + ((c16 ^ g) << 4), kv1 = g * 256 + ((c16 ^ (g | 4)) << 4), kv2 = g * 256 + ((c16 ^ (g | 8)) << 4),
                 kv3 = g * 256 + ((c16 ^ (g | 12)) << 4);
  const unsigned vv0 = g * 256 + ((c16 ^ (g << 1)) << 4), vv1 = g * 256 + ((c16 ^ ((g | 4) << 1)) << 4);
  const unsigned mydst = lds0 + hj * 8192 + rh * 4096;
  int rot = b + 2 * sub + 5 * late; rot = rot % 9;
  auto issue_w = [&](int chunk, int half, int slot) {
    const char* s = wq + (size_t)(chunk * 64 + half * 32 + rh * 16) * 8192;
    const unsigned d = mydst + slot * SLOT;
    dma(s, wv0, d); dma(s + 32768, wv0, d + 1024); dma(s + 65536, wv1, d + 2048); dma(s + 98304, wv1, d + 3072);
  };
  auto issue_x = [&](int chunk, int xs) {
    const char* s = xg + chunk * 128 + w * 24 * 1152;
    const unsigned d = lds0 + XOFF + xs * XSLOT + w * 3072;
    const unsigned a = (w & 1) ? xv1 : xv0, bb = (w & 1) ? xv0 : xv1;
    dma(s, a, d); dma(s + 8 * 1152, bb, d + 1024); dma(s + 16 * 1152, a, d + 2048);
  };
  auto issue_kv = [&](int item, int slot) {
    const unsigned d = mydst + slot * SLOT;
    if (item & 1) dma4_nt(vg + (item >> 1) * 8192, vv0, vv1, vv0, vv1, d);
    else dma4_nt(kg + (item >> 1) * 8192, kv0, kv1, kv2, kv3, d);
  };

  f32x4 acc[8][3];
#pragma unroll
  for (int t = 0; t < 8; ++t)
#pragma unroll
    for (int q = 0; q < 3; ++q) acc[t][q] = f32x4{0.f, 0.f, 0.f, 0.f};

  const int q4 = c16 >> 2, p4 = lane & 3;
  const unsigned wrd = lds0 + hj * 8192 + (8 * g + q4) * 256 + ((q4 | ((g & 1) << 2)) << 5) + 8 * p4;
  const unsigned xrd = lds0 + XOFF + (rh * 48 + c16) * 128 + ((g ^ (c16 >> 1)) << 4);

  auto qstage = [&](int slot, int xs, int ks) {
    bf16x8 xf[3];
    const unsigned xa = (xrd + xs * XSLOT) ^ (ks << 6);
#pragma unroll
    for (int q = 0; q < 3; ++q) xf[q] = *(LAS bf16x8*)(xa + q * 2048);
    const unsigned wa = wrd + slot * SLOT;
#pragma unroll
    for (int t = 0; t < 8; ++t) {
      const unsigned a = wa ^ (t << 5);
      bf16x8 wf = cat8(tr_read(a), tr_read(a + 1024));
#pragma unroll
      for (int q = 0; q < 3; ++q) acc[t][q] = MFMA(wf, xf[q], acc[t][q]);
    }
  };

  {
    int c = rot;
    issue_x(c, 0); issue_w(c, 0, 0); issue_w(c, 1, 1);
    WAIT_BAR(4);
    int slot = 0;
    for (int i = 0; i < 8; ++i) {
      int cn = c + 1; if (cn == 9) cn = 0;
      int s2 = slot + 2; if (s2 >= 3) s2 -= 3;
      int s1 = slot + 1; if (s1 >= 3) s1 -= 3;
      issue_x(cn, (i + 1) & 1); issue_w(cn, 0, s2);
      qstage(slot, i & 1, 0);
      WAIT_BAR(7);
      issue_w(cn, 1, slot);
      qstage(s1, i & 1, 1);
      WAIT_BAR(4);
      slot = s2; c = cn;
    }
    issue_kv(0, 0);
    qstage(1, 0, 0);
    WAIT_BAR(4);
    issue_kv(1, 1);
    qstage(2, 0, 1);
    WAIT_BAR(4);
    issue_kv(2, 2); issue_kv(3, 3);
  }
  bf16x8 qf[4][3];
#pragma unroll
  for (int s = 0; s < 4; ++s)
#pragma unroll
    for (int q = 0; q < 3; ++q) qf[s][q] = pack8(acc[2 * s][q] * QSCALE, acc[2 * s + 1][q] * QSCALE);

#pragma unroll
  for (int t = 0; t < 8; ++t)
#pragma unroll
    for (int q = 0; q < 3; ++q) acc[t][q] = f32x4{0.f, 0.f, 0.f, 0.f};
  float m[3] = {-INFINITY, -INFINITY, -INFINITY}, l[3] = {0.f, 0.f, 0.f};
  const unsigned krd = lds0 + hj * 8192 + c16 * 256 + (((g >> 1) ^ c16) << 4) + 8 * (g & 1);
  const unsigned vrd = lds0 + hj * 8192 + (4 * g + q4) * 256 + ((((4 * g + q4) & 7)) << 5) + 8 * p4;
  bf16x8 pf[3];
  auto qk_half = [&](int slot) {
    const unsigned ka = krd + slot * SLOT;
    f32x4 s[2][3];
#pragma unroll
    for (int kt = 0; kt < 2; ++kt)
#pragma unroll
      for (int q = 0; q < 3; ++q) s[kt][q] = f32x4{0.f, 0.f, 0.f, 0.f};
#pragma unroll
    for (int ks = 0; ks < 4; ++ks) {
      const unsigned a0 = ka ^ (ks << 6), a1 = ka ^ ((ks << 6) | 32);
#pragma unroll
      for (int kt = 0; kt < 2; ++kt) {
        bf16x8 kf = cat8(*(LAS s16x4*)(a0 + kt * 4096), *(LAS s16x4*)(a1 + kt * 4096));
#pragma unroll
        for (int q = 0; q < 3; ++q) s[kt][q] = MFMA(kf, qf[ks][q], s[kt][q]);
      }
    }
    float mn[3]; bool grow = false;
#pragma unroll
    for (int q = 0; q < 3; ++q) {
      float x = fmaxf(fmaxf(fmaxf(s[0][q][0], s[0][q][1]), fmaxf(s[0][q][2], s[0][q][3])),
                      fmaxf(fmaxf(s[1][q][0], s[1][q][1]), fmaxf(s[1][q][2], s[1][q][3])));
      mn[q] = fmaxf(m[q], gmax(x));
      grow |= mn[q] > m[q];
    }
    if (__any(grow)) {
#pragma unroll
      for (int q = 0; q < 3; ++q) {
        const float al = __builtin_amdgcn_exp2f(m[q] - mn[q]);
        l[q] *= al; m[q] = mn[q];
#pragma unroll
        for (int t = 0; t < 8; ++t) acc[t][q] *= al;
      }
    }
#pragma unroll
    for (int q = 0; q < 3; ++q) {
#pragma unroll
      for (int kt = 0; kt < 2; ++kt)
#pragma unroll
        for (int e = 0; e < 4; ++e) { s[kt][q][e] = __builtin_amdgcn_exp2f(s[kt][q][e] - m[q]); l[q] += s[kt][q][e]; }
      pf[q] = pack8(s[0][q], s[1][q]);
    }
  };
  auto pv_half = [&](int slot) {
    const unsigned va = vrd + slot * SLOT;
#pragma unroll
    for (int t = 0; t < 8; ++t) {
      const unsigned a = va ^ (t << 5);
      bf16x8 vf = cat8(tr_read(a), tr_read(a + 4096));
#pragma unroll
      for (int q = 0; q < 3; ++q) acc[t][q] = MFMA(vf, pf[q], acc[t][q]);
    }
  };
  {
    int sl = 0, sf = 4;
    for (int t = 0; t < 14; ++t) {
      issue_kv(2 * t + 4, sf);
      qk_half(sl);
      WAIT_BAR(12);
      sf = sl; sl = sl == 4 ? 0 : sl + 1;
      issue_kv(2 * t + 5, sf);
      pv_half(sl);
      WAIT_BAR(12);
      sf = sl; sl = sl == 4 ? 0 : sl + 1;
    }
    qk_half(3); WAIT_BAR(8);
    pv_half(4); WAIT_BAR(4);
    qk_half(0); WAIT_BAR(0);
    pv_half(1);
  }
  char* og = of + br * OF_BRANCH + ((size_t)head * 4 * 96 + b * 6 + rh * 3) * 1024 + lane * 16;
#pragma unroll
  for (int q = 0; q < 3; ++q) {
    const float inv = 1.0f / gsum(l[q]);
#pragma unroll
    for (int s = 0; s < 4; ++s) *(bf16x8*)(og + (size_t)(s * 96 + q) * 1024) = pack8(acc[2 * s][q] * inv, acc[2 * s + 1][q] * inv);
  }
}

constexpr int LDS_B = 4 * 21 * 1024;
__global__ __launch_bounds__(256, 2) void oproj_kernel(const char* of, const char* woA, const char* woB, char* out) {
  extern __shared__ __attribute__((aligned(1024))) char lds[];
  const unsigned lds0 = (unsigned)(uintptr_t)lds;
  const int lane = threadIdx.x & 63, w = __builtin_amdgcn_readfirstlane(threadIdx.x >> 6);
  const int id = blockIdx.x, xcd = id & 7, r = id >> 3;
  const int br = xcd >> 2, u = (xcd & 3) * 32 + r;
  if (u >= 126) return;
  const int rg = u / 9, ns = u - rg * 9;
  const int qt0 = rg * 7, nq = rg == 13 ? 5 : 7;
  const int g = lane >> 4, c16 = lane & 15, q4 = c16 >> 2, p4 = lane & 3;
  const char* ob = of + br * OF_BRANCH + (size_t)w * 96 * 1024 + lane * 16;
  const char* wb = (br ? woB : woA) + (size_t)(w * 32 + (lane >> 3)) * 1152 + ns * 128 + (lane & 7) * 16;
  const unsigned wbase = lds0 + w * 8192;
  const unsigned wwr = wbase + (lane >> 3) * 128 + (((lane & 7) ^ (((lane >> 4) & 3) << 1)) << 4);
  const unsigned wrd = wbase + (4 * g + q4) * 128 + ((((4 * g + q4) >> 1) & 3) << 5) + 8 * p4;
  int qoff[7];
#pragma unroll
  for (int q = 0; q < 7; ++q) qoff[q] = (qt0 + (q < nq ? q : 0)) * 1024;
  f32x4 acc[4][7];
#pragma unroll
  for (int t = 0; t < 4; ++t)
#pragma unroll
    for (int q = 0; q < 7; ++q) acc[t][q] = f32x4{0.f, 0.f, 0.f, 0.f};
  bf16x8 oA[7], oB[7]; u32x4 wA[4], wB[4];
  auto headof = [&](int i) { return (i & 24) | ((i + u) & 7); };
  auto load = [&](int i, bf16x8* o, u32x4* wv) {
    const int h = headof(i);
    const char* wp = wb + (size_t)h * 128 * 1152;
#pragma unroll
    for (int p = 0; p < 4; ++p) wv[p] = *(const u32x4*)(wp + p * 8 * 1152);
    const char* op = ob + (size_t)h * 4 * 96 * 1024;
#pragma unroll
    for (int q = 0; q < 7; ++q) o[q] = *(const bf16x8*)(op + qoff[q]);
  };
  auto comp = [&](const bf16x8* o, const u32x4* wv, int buf) {
#pragma unroll
    for (int p = 0; p < 4; ++p) *(LAS u32x4*)(wwr + buf * 4096 + p * 1024) = wv[p];
#pragma unroll
    for (int t = 0; t < 4; ++t) {
      const unsigned a = (wrd + buf * 4096) ^ (t << 5);
      bf16x8 wf = cat8(tr_read(a), tr_read(a + 2048));
#pragma unroll
      for (int q = 0; q < 7; ++q) acc[t][q] = MFMA(wf, o[q], acc[t][q]);
    }
  };
  load(0, oA, wA);
  for (int i = 0; i < 32; i += 2) {
    load(i + 1, oB, wB);
    comp(oA, wA, 0);
    if (i + 2 < 32) load(i + 2, oA, wA);
    comp(oB, wB, 1);
  }
  __syncthreads();
#pragma unroll
  for (int t = 0; t < 4; ++t) {
    if (t != w) {
      const int slot = (w - t - 1) & 3;
#pragma unroll
      for (int q = 0; q < 7; ++q) *(LAS f32x4*)(lds0 + ((t * 3 + slot) * 7 + q) * 1024 + lane * 16) = acc[t][q];
    }
  }
  __syncthreads();
  f32x4 res[7];
#pragma unroll
  for (int q = 0; q < 7; ++q) {
    res[q] = w == 0 ? acc[0][q] : w == 1 ? acc[1][q] : w == 2 ? acc[2][q] : acc[3][q];
#pragma unroll
    for (int s = 0; s < 3; ++s) res[q] += *(LAS f32x4*)(lds0 + ((w * 3 + s) * 7 + q) * 1024 + lane * 16);
  }
  char* op = out + (size_t)(qt0 * 16 + c16) * 2304 + br * 1152 + ns * 128 + w * 32 + g * 8;
#pragma unroll
  for (int q = 0; q < 7; ++q)
    if (q < nq) *(u32x2*)(op + (size_t)q * 16 * 2304) = u32x2{pk2(res[q][0], res[q][1]), pk2(res[q][2], res[q][3])};
}

extern "C" void kernel_launch(void* const* d_in, const int* in_sizes, int n_in, void* d_out, int out_size, void* d_ws, size_t ws_size,
                              hipStream_t stream) {
  (void)in_sizes; (void)n_in; (void)out_size; (void)ws_size;
  hipFuncSetAttribute((const void*)attn_kernel, hipFuncAttributeMaxDynamicSharedMemorySize, LDS_A);
  hipFuncSetAttribute((const void*)oproj_kernel, hipFuncAttributeMaxDynamicSharedMemorySize, LDS_B);
  char* of = (char*)d_ws;
  attn_kernel<<<512, 256, LDS_A, stream>>>((const char*)d_in[0], (const char*)d_in[1], (const char*)d_in[2], (const char*)d_in[3],
                                           (const char*)d_in[5], (const char*)d_in[6], (const char*)d_in[7], (const char*)d_in[8], of);
  oproj_kernel<<<256, 256, LDS_B, stream>>>(of, (const char*)d_in[4], (const char*)d_in[9], (char*)d_out);
}
```

```cpp
#include <hip/hip_runtime.h>
#include <hip/hip_bf16.h>
#include <stdint.h>
#include <type_traits>
#include <utility>

typedef __attribute__((ext_vector_type(8))) __bf16 bf16x8;
typedef __attribute__((ext_vector_type(2))) __bf16 bf16x2;
typedef __attribute__((ext_vector_type(4))) short s16x4;
typedef __attribute__((ext_vector_type(4))) float f32x4;
typedef __attribute__((ext_vector_type(2))) float f32x2;
typedef __attribute__((ext_vector_type(4))) unsigned u32x4;
typedef __attribute__((ext_vector_type(2))) unsigned u32x2;
#define LAS __attribute__((address_space(3)))

constexpr int SQ = 96, DM = 576, NH = 32, SK = 512, DH = 128;
constexpr float QSCALE = 0.08838834764831845f * 1.4426950408889634f;
constexpr float THR = 8.0f;

#define MFMA(a, b, c) __builtin_amdgcn_mfma_f32_16x16x32_bf16(a, b, c, 0, 0, 0)
#define LGKM(N) __builtin_amdgcn_s_waitcnt(0xC07F | ((N) << 8))
#define VBAR(N) asm volatile("s_waitcnt vmcnt(%0)\n\ts_barrier" ::"i"(N) : "memory")
#define WAIT_BAR(N) do { LGKM(0); asm volatile("s_waitcnt vmcnt(%0) lgkmcnt(0)\n\ts_barrier" ::"i"(N) : "memory"); } while (0)
#define LBAR() do { LGKM(0); asm volatile("s_waitcnt lgkmcnt(0)\n\ts_barrier" ::: "memory"); } while (0)
#define MFMA_ACC(c, a, b) asm volatile("v_mfma_f32_16x16x32_bf16 %0, %1, %2, %0" : "+v"(c) : "v"(a), "v"(b))
#define MFMA_NEW(c, a, b) asm volatile("v_mfma_f32_16x16x32_bf16 %0, %1, %2, 0" : "=&v"(c) : "v"(a), "v"(b))
#define SB() __builtin_amdgcn_sched_barrier(0)
#define UNR _Pragma("unroll")

#define W_POL ""
#define WS_SHIFT 0
#define ST_POL "sc1"
#define ST_K 38
#define T1_POL "nt"
#define TX_POL "nt"
template <int A, class F, int... I> __device__ __forceinline__ void sfor_(F f, std::integer_sequence<int, I...>) { (f(std::integral_constant<int, A + I>{}), ...); }
template <int A, int B, class F> __device__ __forceinline__ void sfor(F f) { sfor_<A>(f, std::make_integer_sequence<int, B - A>{}); }
__device__ __forceinline__ unsigned spin(const unsigned* p, unsigned tok, unsigned lo) {
  unsigned v = 0;
  _Pragma("clang loop unroll(disable)") for (int it = 0; it < (1 << 18); ++it) {
    v = __hip_atomic_load(p, __ATOMIC_RELAXED, __HIP_MEMORY_SCOPE_AGENT) - tok;
    if (__all(v >= lo && v <= 4u)) break;
    __builtin_amdgcn_s_sleep(1);
  }
  return v;
}
__device__ __forceinline__ void setm0(unsigned ldsbase) { asm volatile("s_mov_b32 m0, %0\n\ts_nop 0" ::"s"(ldsbase) : "memory"); }
template <int OFF> __device__ __forceinline__ void dmai(const char* sbase, unsigned voff) {
  asm volatile("global_load_lds_dwordx4 %0, %1 offset:%2" ::"v"(voff), "s"(sbase), "i"(OFF) : "memory");
}
template <int OFF> __device__ __forceinline__ void dmai_nt(const char* sbase, unsigned voff) {
  asm volatile("global_load_lds_dwordx4 %0, %1 offset:%2 nt" ::"v"(voff), "s"(sbase), "i"(OFF) : "memory");
}
template <int LOFF> __device__ __forceinline__ void st_share(int other, char* sbase, unsigned voff, const u32x4 (&v)[3]) {
  asm volatile("s_cmp_eq_u32 %5, 0\n\ts_cbranch_scc1 1f\n\t"
               "global_store_dwordx4 %0, %1, %4 sc1\n\tglobal_store_dwordx4 %0, %2, %4 offset:1024 sc1\n\tglobal_store_dwordx4 %0, %3, %4 offset:2048 sc1\n\ts_branch 2f\n"
               "1:\n\tds_write_b128 %0, %1 offset:%6\n\tds_write_b128 %0, %2 offset:%7\n\tds_write_b128 %0, %3 offset:%8\n2:\n\ts_nop 0"
               ::"v"(voff), "v"(v[0]), "v"(v[1]), "v"(v[2]), "s"(sbase), "s"(other), "i"(LOFF), "i"(LOFF + 1024), "i"(LOFF + 2048) : "memory", "scc");
}
__device__ __forceinline__ void st_flag(char* sbase, unsigned val) {
  asm volatile("global_store_dword %0, %1, %2 sc1" ::"v"(0u), "v"(val), "s"(sbase) : "memory");
}
__device__ __forceinline__ void feed4(int left, const char* s0, unsigned v0, unsigned v1, unsigned v2, unsigned v3, unsigned d0, const char* s1, unsigned u, unsigned d1) {
  unsigned t;
  asm volatile("s_cmp_lt_i32 %10, 1\n\ts_cbranch_scc1 1f\n\ts_mov_b32 m0, %6\n\ts_nop 0\n\t"
               "global_load_lds_dwordx4 %1, %5 nt\n\tglobal_load_lds_dwordx4 %2, %5 offset:1024 nt\n\t"
               "global_load_lds_dwordx4 %3, %5 offset:2048 nt\n\tglobal_load_lds_dwordx4 %4, %5 offset:3072 nt\n\ts_branch 2f\n"
               "1:\n\ts_mov_b32 m0, %9\n\tv_add_u32 %0, 0x2000, %8\n\tglobal_load_lds_dwordx4 %8, %7\n\tglobal_load_lds_dwordx4 %0, %7 offset:1024\n\t"
               "v_add_u32 %0, 0x4000, %8\n\ts_nop 0\n\tglobal_load_lds_dwordx4 %0, %7 offset:2048\n\tv_add_u32 %0, 0x6000, %8\n\ts_nop 0\n\tglobal_load_lds_dwordx4 %0, %7 offset:3072\n2:"
               : "=&v"(t) : "v"(v0), "v"(v1), "v"(v2), "v"(v3), "s"(s0), "s"(d0), "s"(s1), "v"(u), "s"(d1), "s"(left) : "memory", "scc");
}
__device__ __forceinline__ void ld7_if(int go, u32x4 (&d)[7], const char* sbase, const unsigned (&o)[7]) {
  asm volatile("s_cmp_eq_u32 %15, 0\n\ts_cbranch_scc1 1f\n\t"
               "global_load_dwordx4 %0, %7, %14 sc1\n\tglobal_load_dwordx4 %1, %8, %14 sc1\n\tglobal_load_dwordx4 %2, %9, %14 sc1\n\t"
               "global_load_dwordx4 %3, %10, %14 sc1\n\tglobal_load_dwordx4 %4, %11, %14 sc1\n\tglobal_load_dwordx4 %5, %12, %14 sc1\n\t"
               "global_load_dwordx4 %6, %13, %14 sc1\n1:"
               : "+v"(d[0]), "+v"(d[1]), "+v"(d[2]), "+v"(d[3]), "+v"(d[4]), "+v"(d[5]), "+v"(d[6])
               : "v"(o[0]), "v"(o[1]), "v"(o[2]), "v"(o[3]), "v"(o[4]), "v"(o[5]), "v"(o[6]), "s"(sbase), "s"(go) : "memory", "scc");
}
__device__ __forceinline__ void ld_word(unsigned& d, const char* sbase) {
  asm volatile("global_load_dword %0, %1, %2 sc1" : "=&v"(d) : "v"(0u), "s"(sbase) : "memory");
}
__device__ __forceinline__ void touch(unsigned& sink, const char* sbase, unsigned voff) {
  asm volatile("global_load_dword %0, %1, %2 nt\n\tglobal_load_dword %0, %1, %2 sc1" : "+v"(sink) : "v"(voff), "s"(sbase) : "memory");
}
__device__ __forceinline__ void touch64(unsigned& sink, const char* p) {
  asm volatile("global_load_dword %0, %1, off nt\n\tglobal_load_dword %0, %1, off sc1" : "+v"(sink) : "v"(p) : "memory");
}
__device__ __forceinline__ s16x4 tr_read(unsigned a) { return __builtin_amdgcn_ds_read_tr16_b64_v4i16((LAS s16x4*)a); }
__device__ __forceinline__ bf16x8 cat8(s16x4 lo, s16x4 hi) {
  return __builtin_bit_cast(bf16x8, __builtin_shufflevector(lo, hi, 0, 1, 2, 3, 4, 5, 6, 7));
}
__device__ __forceinline__ unsigned pk2(float a, float b) {
  f32x2 v = {a, b};
  return __builtin_bit_cast(unsigned, __builtin_convertvector(v, bf16x2));
}
__device__ __forceinline__ bf16x8 pack8(f32x4 a, f32x4 b) {
  u32x4 w = {pk2(a[0], a[1]), pk2(a[2], a[3]), pk2(b[0], b[1]), pk2(b[2], b[3])};
  return __builtin_bit_cast(bf16x8, w);
}
__device__ __forceinline__ f32x4 scale4(f32x4 a, float c) {
  float x = a[0] * c, y = a[1] * c, z = a[2] * c, u = a[3] * c;
  asm("" : "+v"(x)); asm("" : "+v"(y)); asm("" : "+v"(z)); asm("" : "+v"(u));
  return f32x4{x, y, z, u};
}
__device__ __forceinline__ float gmax(float x) {
  auto a = __builtin_amdgcn_permlane16_swap(__float_as_uint(x), __float_as_uint(x), false, false);
  x = fmaxf(__uint_as_float(a[0]), __uint_as_float(a[1]));
  auto b = __builtin_amdgcn_permlane32_swap(__float_as_uint(x), __float_as_uint(x), false, false);
  return fmaxf(__uint_as_float(b[0]), __uint_as_float(b[1]));
}
__device__ __forceinline__ float gsum(float x) {
  auto a = __builtin_amdgcn_permlane16_swap(__float_as_uint(x), __float_as_uint(x), false, false);
  x = __uint_as_float(a[0]) + __uint_as_float(a[1]);
  auto b = __builtin_amdgcn_permlane32_swap(__float_as_uint(x), __float_as_uint(x), false, false);
  return __uint_as_float(b[0]) + __uint_as_float(b[1]);
}
__device__ __forceinline__ float max8(f32x4 a, f32x4 b) {
  return fmaxf(fmaxf(fmaxf(fmaxf(a[0], a[1]), a[2]), fmaxf(fmaxf(a[3], b[0]), b[1])), fmaxf(b[2], b[3]));
}

#define TSDECL do {} while (0)
#define TS(k) do {} while (0)
#define TSOUT(base, wg, k0, k1) do {} while (0)

constexpr int SLOT = 16384;
constexpr int WOFF = 5 * SLOT;
constexpr int XOFF = 8 * SLOT;
constexpr int XSLOT = 12288;
constexpr int LDS_A = 160 * 1024;
constexpr int uslot(int s) { return (s % 3 == 0 ? 128 : s % 3 == 1 ? 0 : 80) * 1024; }
constexpr int HOFF = 80 * 1024, PARKOFF = 112 * 1024;
constexpr int nat(int p) { return ((p >> 1) & 1) * 4 + (p >> 2) * 2 + (p & 1); }
constexpr int IMG0 = 112 * 1024, IMG1 = 32 * 1024;
constexpr size_t WS_P = 1u << 20;
constexpr int P_WG = 5 * 8 * 3072 + 128;
#define PRIO_A 2
#define PRIO_G 0
#define OUT_POL "sc1"
#define AV 0
#define AW 8
#define FL_K 37
#define BA 11
#define CA 11
constexpr int endB(int j) { const int v = (j >> 1) * 31 + ((j & 1) ? 31 : 31 - BA); return v > 432 ? 432 : v; }
constexpr int endC(int j) { return (j >> 1) * 32 + ((j & 1) ? 32 : CA); }
constexpr int ivB(int G) { int j = 0; while (endB(j) <= G) ++j; return j; }
constexpr int ivC(int G) { int j = 0; while (endC(j) <= G) ++j; return j; }
constexpr int issW(int s) { return s < 3 ? 0 : ivB(24 * (s - 3) + 10) + 1; }
constexpr int issX(int c) { return c < 2 ? 0 : ivB(24 * (2 * c - 3) + 2) + 1; }
constexpr int issH(int h) { return h < 3 ? 0 : ivC(24 * (h - 3) + 20) + 1; }
constexpr int findW(int j) { for (int s = 2; s < 18; ++s) if (issW(s) == j) return s; return -1; }
constexpr int findX(int j) { for (int c = 1; c < 9; ++c) if (issX(c) == j) return c; return -1; }
constexpr int findH(int j) { for (int h = 2; h < 18; ++h) if (issH(h) == j) return h; return -1; }
constexpr int waitB(int e) {
  int cum = 0, need = -1;
  for (int j = 0; j <= e; ++j) {
    if (findX(j) >= 0) { cum += 3; if (ivB(48 * findX(j) + 1) <= e + 1) need = cum; }
    if (findW(j) >= 0) { cum += 4; if (ivB(24 * findW(j)) <= e + 1) need = cum; }
  }
  return need < 0 ? cum : cum - need;
}
constexpr int waitH(int e) {
  int cum = 0, need = -1;
  for (int j = 0; j <= e; ++j) if (findH(j) >= 0) { cum += 4; if (e == 23 || (findH(j) < 16 && ivC(24 * findH(j)) <= e + 1)) need = cum; }
  return need < 0 ? cum : cum - need;
}
constexpr bool planOk() {
  for (int s = 3; s < 18; ++s) if (issW(s) == issW(s - 1) || issW(s) + 1 >= ivB(24 * s) || issH(s) == issH(s - 1) || issH(s) > 23 || (s < 16 && issH(s) + 1 >= ivC(24 * s))) return false;
  for (int c = 2; c < 9; ++c) if (issX(c) + 1 >= ivB(48 * c + 1)) return false;
  return endB(27) == 432 && endB(26) < 432;
}
static_assert(planOk(), "");
#define PIN(x) asm volatile("" : "+v"(x))
#define MX3(a, b, c) __builtin_fmaxf(__builtin_fmaxf((a), (b)), (c))
constexpr int rslot(int item) { return (65 - item) % 5; }
constexpr int wslotA(int s) { return (s % 6 == 0 ? 64 : s % 6 == 1 ? 48 : s % 6 == 2 ? 32 : s % 6 == 3 ? 80 : s % 6 == 4 ? 112 : 128) * 1024; }
constexpr int xslotA(int c) { return (c % 3 == 0 ? 16 : c % 3 == 1 ? 96 : 144) * 1024; }

__global__ __launch_bounds__(512, 2) void mha_kernel(const char* xA, const char* kA, const char* vA, const char* wqA, const char* woA,
                                                    const char* xB, const char* kB, const char* vB, const char* wqB, const char* woB,
                                                    char* ws, char* out) {
  extern __shared__ __attribute__((aligned(1024))) char lds[];
  unsigned sink;
  {
    const char* pc; unsigned t;
    asm volatile("s_getpc_b64 %1\n9998:\n\ts_lshr_b32 %2, %4, 3\n\ts_lshl_b32 %2, %2, 11\n\tv_and_b32 %0, 15, %3\n\tv_lshl_add_u32 %0, %0, 7, %2\n\t"
                 "v_min_u32 %0, mha_code_end-9998b-4, %0\n\tglobal_load_dword %0, %0, %1"
                 : "=&v"(sink), "=&s"(pc), "=&s"(t) : "v"(threadIdx.x), "s"(blockIdx.x) : "memory", "scc");
    SB();
  }
  TSDECL; TS(0);
  const unsigned lds0 = (unsigned)(uintptr_t)lds;
  const int lane = threadIdx.x & 63, w = __builtin_amdgcn_readfirstlane(threadIdx.x >> 6);
  const int id = blockIdx.x, xcd = id & 7, r = id >> 3;
  const int br = xcd >> 2, b = r >> 1, oct = xcd & 3, sub = r & 1, hg = oct * 2 + sub;
  const int grp = w >> 2, gw = w & 3, hp = gw >> 1, rh = w & 1;
  const char* xg = (br ? xB : xA) + (size_t)b * SQ * DM * 2;
  const char* wqg = (br ? wqB : wqA) + hg * 1024;
  const char* kg = (br ? kB : kA) + ((size_t)b * NH + hg * 4 + hp) * SK * DH * 2 + rh * 4096;
  const char* vg = (br ? vB : vA) + ((size_t)b * NH + hg * 4 + hp) * SK * DH * 2 + rh * 4096;
  const char* wo = (br ? woB : woA) + (size_t)hg * 512 * 1152;
  const int g = lane >> 4, c16 = lane & 15, q4 = c16 >> 2, p4 = lane & 3;
  const f32x4 zero4 = {0.f, 0.f, 0.f, 0.f};

  unsigned mydst = lds0 + gw * 4096;
  unsigned iv0, iv1, iv2, iv3;
  auto item_regs = [&]() { iv0 = g * 256 + ((c16 ^ (g << 1)) << 4); PIN(iv0); iv1 = iv0 ^ 128; iv2 = iv0 ^ 16; iv3 = iv0 ^ 144; };
  auto ipiece = [&](const char* src, int p) {
    if (p == 0) dmai_nt<0>(src, iv0);
    if (p == 1) dmai_nt<1024>(src, iv1);
    if (p == 2) dmai_nt<2048>(src, iv2);
    if (p == 3) dmai_nt<3072>(src, iv3);
  };
  auto item_src = [&](int item) { return ((item & 1) ? vg : kg) + (item >> 5) * 2 * SK * DH * 2 + ((item & 31) >> 1) * 8192; };
  auto issue_at = [&](const char* src, int slot) { setm0(mydst + slot * SLOT); UNR for (int p = 0; p < 4; ++p) ipiece(src, p); };
#define ITEM(i) issue_at(item_src(i), rslot(i))
  const int wg0 = (br * 16 + b) * 8, wgid = wg0 + hg;
  char* myline = ws + wgid * 128;

  f32x4 acc[8][3];
  bf16x8 qf[4][3];
  {
    const unsigned wrd = lds0 + hp * 8192 + (8 * g + q4) * 256 + ((q4 | ((g & 1) << 2)) << 5) + 8 * p4;
    const unsigned xrd = lds0 + (rh * 48 + c16) * 128 + ((g ^ (c16 >> 1)) << 4);
    bf16x8 wf[8], xf[2][3];
    auto qinit = [&]() {
      UNR for (int t = 0; t < 8; ++t) UNR for (int q = 0; q < 3; ++q) { acc[t][q] = zero4; PIN(acc[t][q]); }
      UNR for (int t = 5; t < 8; ++t) { wf[t] = bf16x8{}; PIN(wf[t]); }
      UNR for (int q = 0; q < 3; ++q) { xf[1][q] = bf16x8{}; PIN(xf[1][q]); }
    };
    auto qstage = [&](auto parc, auto wad, unsigned xa, auto hook) {
      constexpr int par = decltype(parc)::value;
      sfor<0, 24>([&](auto kc) {
        constexpr int k = decltype(kc)::value, t = (k / 3 + 5) & 7, q = k % 3, pp = k < 9 ? par ^ 1 : par;
        if (k == 9) LGKM(8);
        if (k == 15) LGKM(8);
        if (k == 21) LGKM(0);
        SB();
        MFMA_ACC(acc[t][q], wf[t], xf[pp][q]);
        SB();
        const int u = k == 0 ? 0 : k == 3 ? 1 : k == 4 ? 2 : k == 5 ? 3 : k == 6 ? 4 : k == 7 ? 5 : k == 9 ? 6 : k == 10 ? 7 : -1;
        if (u >= 0) { const unsigned a = wad(u); wf[u & 7] = cat8(tr_read(a), tr_read(a + 1024)); }
        if (k == 1) { xf[par][0] = *(LAS bf16x8*)(xa); xf[par][1] = *(LAS bf16x8*)(xa + 2048); }
        if (k == 2) xf[par][2] = *(LAS bf16x8*)(xa + 4096);
        SB();
        hook(kc);
        SB();
      });
    };
    auto qfinish = [&](int half, bool single) {
      float qs = QSCALE;
      asm volatile("" : "+s"(qs));
      if (half == 0) asm volatile("s_nop 7" : "+v"(acc[4][0]), "+v"(acc[4][1]), "+v"(acc[4][2]), "+v"(acc[3][0]), "+v"(acc[3][1]), "+v"(acc[3][2]), "+v"(acc[2][1]), "+v"(acc[2][2]));
      if (half == 0) { UNR for (int t = 5; t < 8; ++t) UNR for (int q = 0; q < 3; ++q) { acc[t][q] = MFMA(wf[t], xf[1][q], acc[t][q]); asm volatile("" :: "v"(acc[t][q])); } }
      UNR for (int s = 2 * half; s < 2 * half + 2; ++s)
        UNR for (int q = 0; q < 3; ++q) {
          const f32x4 a = single ? scale4(acc[2 * s][q], qs) : acc[2 * s][q] * qs, c = single ? scale4(acc[2 * s + 1][q], qs) : acc[2 * s + 1][q] * qs;
          auto lo = __builtin_amdgcn_permlane16_swap(pk2(a[0], a[1]), pk2(c[0], c[1]), false, false);
          auto hi = __builtin_amdgcn_permlane16_swap(pk2(a[2], a[3]), pk2(c[2], c[3]), false, false);
          qf[s][q] = __builtin_bit_cast(bf16x8, u32x4{lo[0], hi[0], lo[1], hi[1]});
          PIN(qf[s][q]);
        }
    };
    std::integral_constant<int, 0> P0; std::integral_constant<int, 1> P1;
    auto nohook = [](auto) {};
    if (grp == 0) {
      unsigned wlo[8], whi[8], xlo[2], xhi[2];
      UNR for (int t = 0; t < 8; ++t) { wlo[t] = (wrd ^ (t << 5)) + 32 * 1024; whi[t] = wlo[t] + 80 * 1024; PIN(wlo[t]); PIN(whi[t]); }
      UNR for (int k = 0; k < 2; ++k) { xlo[k] = xrd ^ (k << 6); xhi[k] = xlo[k] + 96 * 1024; PIN(xlo[k]); PIN(xhi[k]); }
      {
        const char* ta = p4 == 0 ? wo : p4 == 1 ? out + (size_t)(b * 96) * 2304 : ws + WS_P + (size_t)(wg0 + gw * 2 + p4 - 1) * P_WG - 128;
        asm volatile("global_load_dword %0, %1, off sc1" : "+v"(sink) : "v"(ta) : "memory");
      }
      unsigned tok0;
      ld_word(tok0, myline + 64);
      qinit();
      LBAR();
      TS(1);
      for (int n = 0; n < 3; ++n) {
        if (w == 0 && n > 0) {
          asm volatile("s_waitcnt vmcnt(0)" : "+v"(tok0) :: "memory");
          asm volatile("global_store_dword %0, %1, %2 sc1" ::"v"(n == 1 ? (lane & 7) * 4 : 64), "v"(n == 1 ? tok0 : tok0 + 16), "s"(myline) : "memory");
        }
        UNR for (int s = 0; s < 6; ++s) {
          auto wad = [&](int t) { return s < 4 ? wlo[t] + (wslotA(s) - 32 * 1024) : whi[t] + (wslotA(s) - 112 * 1024); };
          const unsigned xa = s < 2 ? xlo[s & 1] + xslotA(0) : xhi[s & 1] + (xslotA(s >> 1) - 96 * 1024);
          if (s & 1) qstage(P1, wad, xa, nohook); else qstage(P0, wad, xa, nohook);
          LBAR();
        }
      }
      TS(2);
      asm volatile("s_waitcnt vmcnt(0)" : "+v"(sink), "+v"(tok0) :: "memory");
      qfinish(0, false); qfinish(1, false);
    } else {
      const unsigned wv0 = g * 8192 + ((c16 ^ (g << 1)) << 4), wv1 = wv0 + 31744, wv2 = (wv0 ^ 128) + 2 * 31744, wv3 = (wv0 ^ 128) + 3 * 31744;
      const unsigned xv0 = (lane >> 3) * 1152 + (((lane & 7) ^ (g | ((gw & 1) << 2))) << 4), xv1 = (xv0 ^ 64) + 8192, xv2 = xv0 + 16384;
      const char* wq = wqg + hp * 256 + (size_t)rh * 16 * 8192;
      const char* xw = xg + gw * 24 * 1152;
      unsigned xdst = lds0 + gw * 3072;
      item_regs();
      auto wpiece = [&](const char* s, int p) {
        if (p == 0) dmai<0>(s, wv0);
        if (p == 1) dmai<1024>(s, wv1);
        if (p == 2) dmai<2048>(s, wv2);
        if (p == 3) dmai<3072>(s, wv3);
      };
      auto xpiece = [&](const char* s, int p) {
        if (p == 0) dmai<0>(s, xv0);
        if (p == 1) dmai<1024>(s, xv1);
        if (p == 2) dmai<2048>(s, xv2);
      };
      auto xchunk = [&](int chunk, int xo) { setm0(xdst + xo); UNR for (int p = 0; p < 3; ++p) xpiece(xw + chunk * 128, p); };
      auto wstage = [&](int pair, int st, int wo_) { setm0(mydst + wo_); UNR for (int p = 0; p < 4; ++p) wpiece(wq + pair * 512 + (size_t)st * 32 * 8192, p); };
      auto warm = [&](int part) {
        if (part < 4) {
          const int per = part ? 8 : 10, n = gw * per + min(max(lane - 24, 0), per - 1);
          const char* px = xg + (sub * 48 + gw * 12 + (lane >> 1)) * 1152 + (1 + 2 * part + (lane & 1)) * 128;
          const char* pw = wqg + (size_t)((part ? 64 + 128 * part : 32) + (n >> 2) * 16 + b) * 8192 + (n & 3) * 128;
          touch64(sink, lane < 24 ? px : pw);
        } else {
          const int n = gw * 36 + min(lane, 35);
          touch(sink, wqg + 512, (unsigned)((n >> 2) * 16 + b) * 8192 + (n & 3) * 128);
        }
      };
      auto wowarm = [&]() { touch(sink, wo + b * 16 * 1152, (gw * 36 + min(lane, 35)) * 128); };
#define FRESH() asm volatile("" : "+s"(xw), "+s"(wq), "+s"(xdst), "+s"(mydst), "+s"(kg), "+s"(vg))
#define XA(c) xchunk(c, xslotA(c))
#define WA(s) wstage(0, s, wslotA(s))
#define XB(c) do { xchunk(c, XOFF + (c) * XSLOT); qinit(); } while (0)
#define WB(s) wstage(1, s, WOFF + (s) * SLOT)
      XA(0); WA(0); warm(0); WAIT_BAR(2);
      WA(1); XA(1); WA(2); WA(3); WAIT_BAR(11);
      XA(2); WA(4); WA(5); warm(1); WAIT_BAR(17);
      warm(2); WAIT_BAR(15);
      WAIT_BAR(8);
      FRESH(); XA(3); WA(6); WA(7); WAIT_BAR(15);
      XA(4); WA(8); WA(9); WAIT_BAR(15);
      warm(3); ITEM(0); WAIT_BAR(17);
      WAIT_BAR(10);
      FRESH(); XA(5); WA(10); WA(11); WAIT_BAR(17);
      XA(6); WA(12); WA(13); WAIT_BAR(15);
      warm(4); WAIT_BAR(13);
      WAIT_BAR(6);
      FRESH(); XA(7); WA(14); WA(15); WAIT_BAR(13);
      XA(8); WA(16); WA(17); WAIT_BAR(15);
      ITEM(1); ITEM(2); WAIT_BAR(19);
      WAIT_BAR(12);
      ITEM(3); WAIT_BAR(12);
      WAIT_BAR(58);
      FRESH(); WB(0); WB(1); XB(0); WAIT_BAR(0);
      __builtin_amdgcn_s_setprio(PRIO_G);
      unsigned wb[8], xb[2];
      UNR for (int t = 0; t < 8; ++t) { wb[t] = (wrd ^ (t << 5)) + WOFF; PIN(wb[t]); }
      UNR for (int k = 0; k < 2; ++k) { xb[k] = (xrd ^ (k << 6)) + XOFF; PIN(xb[k]); }
      sfor<0, 18>([&](auto sc) {
        constexpr int st = decltype(sc)::value;
        asm volatile("" : "+s"(xw), "+s"(wq), "+s"(xdst), "+s"(mydst));
        qstage(std::integral_constant<int, (st & 1)>{}, [&](int t) { return wb[t] + (st % 3) * SLOT; }, xb[st & 1] + ((st >> 1) & 1) * XSLOT, [&](auto kc) {
          constexpr int G = 24 * st + decltype(kc)::value, j = ivB(G), o = G - (j ? endB(j - 1) : 0), c = findX(j), sw = findW(j), ow = c >= 0 ? 3 : 0;
          if (c >= 0 && o < 3) { if (o == 0) setm0(xdst + XOFF + (c & 1) * XSLOT); xpiece(xw + c * 128, o); }
          if (sw >= 0 && o >= ow && o < ow + 4) { if (o == ow) setm0(mydst + WOFF + (sw % 3) * SLOT); wpiece(wq + 512 + (size_t)sw * 32 * 8192, o - ow); }
          if (j == 26 && o == 0) wowarm();
          if (G + 1 == endB(j)) { constexpr int n_ = j >= 26 ? 63 : waitB(j); WAIT_BAR(n_); }
        });
      });
      qfinish(0, true); LBAR();
      qfinish(1, true); WAIT_BAR(0);
      PIN(sink);
    }
  }

  unsigned uv = (lane >> 3) * 1152 + (((lane & 7) ^ (g << 1)) << 4);
  f32x4 l4[3];
  {
    f32x4 nm[3];
    bf16x8 vf[8];
    u32x4 pw[3] = {};
    u32x4 ones = {0x3f803f80u, 0x3f803f80u, 0x3f803f80u, 0x3f803f80u};
    PIN(ones);
    UNR for (int t = 0; t < 8; ++t) UNR for (int q = 0; q < 3; ++q) { acc[t][q] = zero4; PIN(acc[t][q]); }
    UNR for (int q = 0; q < 3; ++q) { nm[q] = zero4; PIN(nm[q]); l4[q] = zero4; PIN(l4[q]); }
    UNR for (int t = 0; t < 8; ++t) { vf[t] = bf16x8{}; PIN(vf[t]); }
    const unsigned krd = lds0 + hp * 8192 + c16 * 256 + (((((g & 1) << 1) | (g >> 1)) ^ (((c16 & 7) << 1) | (c16 >> 3))) << 4);
    const unsigned vrd = lds0 + hp * 8192 + (4 * g + q4) * 256 + (((4 * g + q4) & 7) << 5) + (((p4 >> 1) ^ (g >> 1)) << 4) + 8 * (p4 & 1);
    item_regs();
    PIN(uv);
    const char* kc = grp ? item_src(36) : item_src(4);
    const char* vc = grp ? item_src(37) : item_src(5);
    const char* usrc = wo + (256 + gw * 32) * 1152 + 8 * 128;
    unsigned fdst = lds0 + gw * 4096;
    asm volatile("" : "+s"(fdst));
    unsigned udst = fdst + uslot(0), fo = (grp ? rslot(36) : rslot(4)) * SLOT;
    int left = grp ? 14 : 16;
    if (grp == 1) LBAR();
    const unsigned top = 4 * SLOT;
    unsigned ro = (grp ? rslot(32) : rslot(0)) * SLOT;
#define RSTEP() do { ro = ro == 0 ? top : ro - SLOT; asm volatile("" : "+s"(ro)); } while (0)
#define pf_(q) __builtin_bit_cast(bf16x8, pw[q])
#define USE(x) asm volatile("" :: "v"(x))
#define PV1(t, q) do { acc[t][q] = MFMA(vf[t], pf_(q), acc[t][q]); USE(acc[t][q]); } while (0)
#define LS1(q) do { l4[q] = MFMA(__builtin_bit_cast(bf16x8, ones), pf_(q), l4[q]); USE(l4[q]); } while (0)
#define QK1(q, k) do { s[(k) & 1][q] = MFMA(kf[(k) >> 1][(k) & 1], qf[(k) >> 1][q], (k) > 1 ? s[(k) & 1][q] : nm[q]); USE(s[(k) & 1][q]); } while (0)
#define EX1(q, j) do { const float e_ = __builtin_amdgcn_exp2f(s[(j) >> 2][q][(j) & 3]); USE(e_); s[(j) >> 2][q][(j) & 3] = e_; } while (0)
#define CV1(q, j) do { const unsigned c_ = pk2(s[(j) >> 1][q][((j) & 1) * 2], s[(j) >> 1][q][((j) & 1) * 2 + 1]); USE(c_); pw[q][j] = c_; } while (0)
#define MAXA(q) do { m0 = MX3(s[0][q][0], s[0][q][1], s[0][q][2]); m1 = MX3(s[0][q][3], s[1][q][0], s[1][q][1]); } while (0)
#define MAXB(q) do { m0 = MX3(m0, s[1][q][2], s[1][q][3]); m0 = MX3(m0, m1, ninf); } while (0)
#define RV(k, a) vf[k] = cat8(tr_read(a), tr_read((a) + 4096))
#define GUARD(q) do { if (__builtin_expect(bad != 0, 0)) { \
      const float d = fmaxf(gmax(m0), dmin), al = __builtin_amdgcn_exp2f(-fmaxf(d, 0.f)); \
      nm[q] -= d; s[0][q] -= d; s[1][q] -= d; \
      if (t > 0) { l4[q] *= al; UNR for (int u = 0; u < 8; ++u) acc[u][q] *= al; } } SB(); } while (0)
#define ABAR() do { __builtin_amdgcn_s_waitcnt(0x0070 | AW); asm volatile("s_barrier" ::: "memory"); } while (0)
#define FSTEP() do { fo = fo == 0 ? top : fo - SLOT; asm volatile("" : "+s"(fo)); } while (0)
    __builtin_amdgcn_s_setprio(PRIO_A);
    float ninf = -INFINITY, thr = -INFINITY, dmin = -INFINITY;
    asm volatile("" : "+s"(ninf));
    _Pragma("clang loop unroll(disable)") for (int t = 0; t < 16; ++t) {
      asm volatile("" : "+s"(thr), "+s"(dmin));
      bf16x8 kf[4][2];
      f32x4 s[2][3];
      float m0, m1;
      unsigned long long bad;
      unsigned ka = 0, a0 = 0, a1 = 0, a2 = 0;
      UNR for (int k = 0; k < 8; ++k) {
        PV1(k, 2); SB();
        if (k == 0) { ka = krd + ro; a0 = ka ^ 64; PIN(ka); PIN(a0); }
        if (k == 1) { kf[0][0] = *(LAS bf16x8*)(ka); kf[0][1] = *(LAS bf16x8*)(ka + 4096); }
        if (k == 2) { kf[1][0] = *(LAS bf16x8*)(a0); kf[1][1] = *(LAS bf16x8*)(a0 + 4096); }
        if (k == 3) { a0 = ka ^ 128; a1 = ka ^ 192; PIN(a0); PIN(a1); }
        if (k == 4) { kf[2][0] = *(LAS bf16x8*)(a0); kf[2][1] = *(LAS bf16x8*)(a0 + 4096); }
        if (k == 5) { kf[3][0] = *(LAS bf16x8*)(a1); kf[3][1] = *(LAS bf16x8*)(a1 + 4096); }
        if (k == 6) RSTEP();
        SB();
      }
      LS1(2); SB(); LGKM(4); SB();
      UNR for (int k = 0; k < 8; ++k) {
        if (k == 4) { LGKM(0); SB(); }
        QK1(0, k); SB();
        if (k == 0) { ka = vrd + ro; a0 = ka ^ 32; PIN(ka); PIN(a0); }
        if (k == 1) { a1 = ka ^ 64; a2 = ka ^ 96; PIN(a1); PIN(a2); }
        if (k == 5) RSTEP();
        SB();
      }
      QK1(1, 0); QK1(1, 1); SB();
      QK1(1, 2); SB(); MAXA(0); SB();
      QK1(1, 3); SB(); MAXB(0); bad = __ballot(!(m0 <= thr)); SB();
      QK1(1, 4); SB();
      GUARD(0);
      EX1(0, 0); SB();
      QK1(1, 5); SB(); EX1(0, 1); SB();
      QK1(1, 6); SB(); EX1(0, 2); CV1(0, 0); SB();
      QK1(1, 7); SB(); EX1(0, 3); SB();
      QK1(2, 0); SB(); EX1(0, 4); CV1(0, 1); SB();
      QK1(2, 1); SB(); EX1(0, 5); SB();
      QK1(2, 2); SB(); EX1(0, 6); CV1(0, 2); MAXA(1); SB();
      QK1(2, 3); SB(); EX1(0, 7); MAXB(1); CV1(0, 3); bad = __ballot(!(m0 <= thr)); SB();
      feed4(left, kc, iv0, iv1, iv2, iv3, fdst + fo, usrc, uv, udst);
      ABAR();
      RV(0, ka); SB();
      GUARD(1);
      QK1(2, 4); SB(); RV(1, a0); a0 ^= 128; EX1(1, 0); SB();
      QK1(2, 5); SB(); RV(2, a1); a1 ^= 128; EX1(1, 1); SB();
      QK1(2, 6); SB(); RV(3, a2); a2 ^= 128; SB();
      QK1(2, 7); SB(); ka ^= 128; RV(4, ka); SB();
      LS1(0); SB(); RV(5, a0); LGKM(8); SB();
      PV1(0, 0); SB(); RV(6, a1); SB();
      PV1(1, 0); SB(); RV(7, a2); LGKM(8); SB();
      PV1(2, 0); SB(); EX1(1, 2); CV1(1, 0); SB();
      PV1(3, 0); SB(); EX1(1, 3); MAXA(2); LGKM(4); SB();
      PV1(4, 0); SB(); EX1(1, 4); CV1(1, 1); SB();
      PV1(5, 0); SB(); EX1(1, 5); MAXB(2); LGKM(0); SB();
      PV1(6, 0); SB(); EX1(1, 6); CV1(1, 2); bad = __ballot(!(m0 <= thr)); SB();
      PV1(7, 0); SB(); EX1(1, 7); SB();
      GUARD(2);
      CV1(1, 3); EX1(2, 0); SB();
      LS1(1); SB(); EX1(2, 1); SB();
      UNR for (int k = 0; k < 8; ++k) {
        PV1(k, 1); SB();
        if (k < 6) EX1(2, k + 2);
        if (!(k & 1)) CV1(2, k >> 1);
        SB();
      }
      thr = THR; dmin = 0.f;
      FSTEP();
      feed4(left, vc, iv0, iv1, iv2, iv3, fdst + fo, usrc + 128 * 1152, uv, udst + 16384);
      ABAR();
      FSTEP();
      { const int inc = t == 13 ? 8192 + SK * DH * 2 : 8192; kc += inc; vc += inc; }
      if (t == 14) { usrc -= 8 * 128; udst += uslot(1) - uslot(0); }
      --left;
    }
    UNR for (int k = 0; k < 8; ++k) PV1(k, 2);
    LS1(2);
  }
  TS(4);

  unsigned l16 = lane * 16;
  auto wpc = [&](const char* src, unsigned dst, int p) {
    if (p == 0) { setm0(dst); dmai<0>(src, uv); }
    if (p == 1) dmai<1024>(src + 8192, uv);
    if (p == 2) dmai<2048>(src + 16384, uv);
    if (p == 3) dmai<3072>(src + 24576, uv);
  };
  const char* wsrc = wo + gw * 32 * 1152;
  unsigned hdst = lds0 + gw * 4096;
  auto hpiece = [&](int h, int p) { wpc(wsrc + (h & 1) * 128 * 1152 + (h >> 1) * 128, hdst + HOFF + (h % 3) * 16384, p); };
  if (grp == 0) __builtin_amdgcn_s_setprio(PRIO_G);
  {
    unsigned na = lds0 + (grp ? IMG1 : IMG0) + (hp * 24 + rh * 3) * 1024 + l16;
    PIN(na);
    sfor<0, 6>([&](auto jc) {
      constexpr int j = decltype(jc)::value, q = j % 3, s0 = j / 3 * 2;
      if (grp == 0 && j >= 4) { UNR for (int p = 0; p < 4; ++p) hpiece(j - 4, p); }
      SB();
      const float inv = __builtin_amdgcn_rcpf(l4[q][0]);
      const bf16x8 v0 = pack8(scale4(acc[2 * s0][q], inv), scale4(acc[2 * s0 + 1][q], inv)), v1 = pack8(scale4(acc[2 * s0 + 2][q], inv), scale4(acc[2 * s0 + 3][q], inv));
      *(LAS bf16x8*)(na + s0 * 6144 + q * 1024) = v0;
      *(LAS bf16x8*)(na + s0 * 6144 + 6144 + q * 1024) = v1;
      if (grp == 0 || q == 2) WAIT_BAR(j < 3 ? 8 - 4 * j : 63);
    });
  }
  int ln;
  if (grp == 1) {
    __builtin_amdgcn_s_setprio(0);
    UNR for (int p = 0; p < 8; ++p) wpc(wo + (256 + gw * 64 + (p >> 2) * 32) * 1152 + 128, lds0 + uslot(2) + gw * 8192 + (p >> 2) * 4096, p & 3);
    unsigned tme, fl = 0;
    ld_word(tme, myline + 64);
    const int par = hg & 1, nr = gw < 3 ? 5 - par : 0, u = gw * 64 + lane;
    const char* psrc = ws + WS_P + (size_t)wgid * P_WG;
    char* o = out + (size_t)(b * 96 + ((hg >> 1) & 1) * 48 + (u >> 6) * 16 + (u & 15)) * 2304 + br * 1152 + (hg >> 2) * 64 + (((g & 1) << 1) | (g >> 1)) * 16;
    u32x4 va[7], vb[7];
    UNR for (int h = 0; h < 7; ++h) asm volatile("" : "=v"(va[h]), "=v"(vb[h]));
    int nxt = 0, ra = -1, rb = -1;
    unsigned po[7];
    UNR for (int h = 0; h < 7; ++h) po[h] = ((hg + 1 + h) & 7) * 3072 + u * 16;
    UNR for (int h = 0; h < 7; ++h) PIN(po[h]);
    LBAR();
    auto fetch_if = [&](bool c, u32x4 (&v)[7], int& r_) {
      const int go = __builtin_amdgcn_readfirstlane(c ? 1 : 0);
      ld7_if(go, v, psrc + nxt * 24576, po);
      r_ = go ? nxt : -1; nxt += go;
    };
#define LANDED(v) asm volatile("" : "+v"(v[0]), "+v"(v[1]), "+v"(v[2]), "+v"(v[3]), "+v"(v[4]), "+v"(v[5]), "+v"(v[6]))
    auto sum = [&](u32x4 (&v)[7], int& r_) {
      const u32x4 m = *(volatile LAS u32x4*)(lds0 + IMG1 + r_ * 3072 + u * 16);
      float t[8];
      UNR for (int e = 0; e < 4; ++e) { t[2 * e] = __uint_as_float(m[e] << 16); t[2 * e + 1] = __uint_as_float(m[e] & 0xffff0000u); }
      UNR for (int h = 0; h < 7; ++h)
        UNR for (int e = 0; e < 4; ++e) { t[2 * e] += __uint_as_float(v[h][e] << 16); asm("" : "+v"(t[2 * e])); t[2 * e + 1] += __uint_as_float(v[h][e] & 0xffff0000u); asm("" : "+v"(t[2 * e + 1])); }
      char* oo = o + (par ? 2 * r_ + 1 : r_ == 0 ? 8 : 2 * r_ - 2) * 128;
      auto lo = __builtin_amdgcn_permlane16_swap(pk2(t[0], t[1]), pk2(t[4], t[5]), false, false);
      auto hi = __builtin_amdgcn_permlane16_swap(pk2(t[2], t[3]), pk2(t[6], t[7]), false, false);
      asm volatile("global_store_dwordx4 %0, %1, off " OUT_POL "\n\ts_nop 1" ::"v"(oo), "v"(u32x4{lo[0], hi[0], lo[1], hi[1]}) : "memory");
    };
    const char* src = wo + (256 + gw * 64) * 1152 + 256;
    unsigned d0 = lds0 + uslot(0) + gw * 8192, d1 = lds0 + uslot(1) + gw * 8192, d2 = lds0 + uslot(2) + gw * 8192, fm = 0;
    auto line = [&](int i, unsigned& fnew, unsigned& fold, u32x4 (&v)[7], int& r_, int rother) {
      asm volatile("global_load_dword %0, %1, %2 sc1" : "+v"(fnew) : "v"((lane & 7) * 4), "s"(myline) : "memory");
      if (i < 7) {
        UNR for (int p = 0; p < 8; ++p) wpc(src + (p >> 2) * 32 * 1152, d0 + (p >> 2) * 4096, p & 3);
        if (rother >= 0) asm volatile("s_waitcnt vmcnt(16)" ::: "memory"); else asm volatile("s_waitcnt vmcnt(9)" ::: "memory");
      } else if (rother >= 0) asm volatile("s_waitcnt vmcnt(8)" ::: "memory"); else asm volatile("s_waitcnt vmcnt(1)" ::: "memory");
      src += 128; { const unsigned t = d0; d0 = d1; d1 = d2; d2 = t; }
      asm volatile("" : "+v"(tme), "+v"(fold));
      LANDED(v);
      if (r_ >= 0) sum(v, r_);
      fetch_if(i > 1 && nxt < nr && __all(fold - tme >= (unsigned)nxt && fold - tme <= 4u), v, r_);
      if (i < 8) LBAR();
    };
    _Pragma("clang loop unroll(disable)") for (int i = 1; i < 9; i += 2) { line(i, fl, fm, va, ra, rb); line(i + 1, fm, fl, vb, rb, ra); }
    _Pragma("clang loop unroll(disable)") while (ra >= 0 || rb >= 0 || nxt < nr) {
      asm volatile("s_waitcnt vmcnt(0)" : "+v"(fl), "+v"(fm) :: "memory");
      LANDED(va); LANDED(vb);
      if (ra >= 0) sum(va, ra);
      if (rb >= 0) sum(vb, rb);
      unsigned f = 0;
      if (nxt < nr) f = spin((const unsigned*)myline + (lane & 7), tme, nxt);
      fetch_if(nxt < nr, va, ra);
      fetch_if(nxt < nr && __all(f >= (unsigned)nxt && f <= 4u), vb, rb);
    }
    asm volatile("s_waitcnt vmcnt(0)\n\ts_endpgm" : "+v"(fl), "+v"(fm) :: "memory");
  }
  {
    const int qh = gw & 1, nh = gw >> 1;
    bf16x8 of[8][3];
    unsigned ia = lds0 + IMG0 + qh * 3072 + l16;
    PIN(ia);
    char* peer = ws + (wg0 + gw * 2) * 128;
    unsigned tp0, tp1;
    ld_word(tp0, peer + 64); ld_word(tp1, peer + 192);
    UNR for (int f = 0; f < 12; ++f) of[f / 3][f % 3] = *(LAS bf16x8*)(ia + (f / 3) * 6144 + (f % 3) * 1024);
    LBAR();
    UNR for (int f = 12; f < 24; ++f) of[f / 3][f % 3] = *(LAS bf16x8*)(ia + (f / 3) * 6144 + (f % 3) * 1024);
    WAIT_BAR(0);
    unsigned sink2 = 0;
    f32x4 oc[2][2][3];
    u32x4 hp_[8][3];
    bf16x8 wf[8];
    unsigned b0 = lds0 + HOFF + (4 * g + q4) * 128 + (((2 * nh) ^ (((4 * g + q4) >> 1) & 3)) << 5) + 8 * p4, b1 = b0 ^ 32;
    PIN(b0); PIN(b1);
    auto hstage = [&](int h, int off, auto hook) {
      sfor<0, 24>([&](auto kc) {
        constexpr int k = decltype(kc)::value;
        const int f = (k / 3 + 5) & 7, q = k % 3, hh = k < 9 ? h - 1 : h, set = hh < 18 ? 0 : ((hh - 18) >> 1) & 1;
        if (k == 9 && h > 0 && h < 18 && !(h & 1)) {
          UNR for (int u = 0; u < 3; ++u) {
            hp_[h / 2 - 1][u] = u32x4{pk2(oc[0][0][u][0], oc[0][0][u][1]), pk2(oc[0][0][u][2], oc[0][0][u][3]), pk2(oc[0][1][u][0], oc[0][1][u][1]), pk2(oc[0][1][u][2], oc[0][1][u][3])};
            PIN(hp_[h / 2 - 1][u]);
          }
          SB();
        }
        if (hh >= 0) oc[set][f & 1][q] = MFMA(wf[f], of[(hh & 1) * 4 + (f >> 1)][q], (hh < 18 && !(hh & 1) && f < 2) ? zero4 : oc[set][f & 1][q]);
        SB();
        const int u = k == 0 ? 0 : k == 3 ? 1 : k == 6 ? 2 : k == 9 ? 3 : k == 12 ? 4 : k == 15 ? 5 : k == 18 ? 6 : k == 20 ? 7 : -1;
        if (u >= 0) { const unsigned a = ((u & 1) ? b1 : b0) + (h < 18 ? off + (u >> 1) * 4096 : nat((h & 1) * 4 + (u >> 1)) * 4096); wf[u & 7] = cat8(tr_read(a), tr_read(a + 2048)); }
        SB();
        hook(kc);
        SB();
      });
    };
#define FRESH2() asm volatile("" : "+s"(wsrc), "+s"(hdst))
    asm volatile("" : "+v"(tp0), "+v"(tp1));
    tp0 = __builtin_amdgcn_readfirstlane(tp0); tp1 = __builtin_amdgcn_readfirstlane(tp1);
    asm volatile("" : "+s"(tp0), "+s"(tp1));
    sfor<0, 16>([&](auto hc) {
      constexpr int h = decltype(hc)::value;
      FRESH2();
      hstage(h, (h % 3) * 16384, [&](auto kc) {
        constexpr int G = 24 * h + decltype(kc)::value, j = ivC(G), o = G - (j ? endC(j - 1) : 0), hn = findH(j);
        if (hn >= 0 && o < 8 && !(o & 1)) hpiece(hn, o >> 1);
        if (j == 8 && o == 8) touch(sink2, wo + (256 + b * 16) * 1152, gw * 36 * 128 + min(l16, 560u) * 8);
        if (G + 1 == endC(j)) { constexpr int n_ = waitH(j); WAIT_BAR(n_); }
      });
      if (h == 11) PIN(sink2);
    });
    TS(5);
    hstage(16, 16384, [&](auto) {});
    hstage(17, 32768, [&](auto) {});
    LBAR();
    TS(6);
    char* pdst = ws + WS_P + (size_t)(wg0 + gw * 2) * P_WG + hg * 3072;
    u32x4 pk[3];
    ia -= IMG0 - IMG1;
    auto pack = [&](int set, int j) { pk[j >> 1][(j & 1) * 2] = pk2(oc[set][j & 1][j >> 1][0], oc[set][j & 1][j >> 1][1]);
                                      pk[j >> 1][(j & 1) * 2 + 1] = pk2(oc[set][j & 1][j >> 1][2], oc[set][j & 1][j >> 1][3]); PIN(pk[j >> 1]); };
    const int oth0 = hg - 2 * gw, oth1 = oth0 - 1;
    auto pstore = [&](auto ic) {
      constexpr int i = decltype(ic)::value, odd = i && !(i & 1), rr = odd ? i / 2 - 1 : (i + 1) / 2;
      st_share<IMG1 + rr * 3072>(odd ? oth1 : oth0, pdst + (odd * P_WG + rr * 24576), l16, pk);
    };
    auto flag = [&](int i) { if (i && !(i & 1)) st_flag(peer + 128 + hg * 4, tp1 + i / 2 - 1); else st_flag(peer + hg * 4, tp0 + (i + 1) / 2); };
    auto img = [&](int f) { of[f / 3][f % 3] = *(LAS bf16x8*)(ia + nat(f / 3) * 6144 + (f % 3) * 1024); };
    img(0); img(1); img(2);
    sfor<0, 9>([&](auto ic) {
      constexpr int i = decltype(ic)::value;
      asm volatile("" : "+s"(pdst), "+s"(peer));
      b0 += uslot(i) - (i ? uslot(i - 1) : HOFF); b1 = b0 ^ 32;
      PIN(b0); PIN(b1);
      auto xch = [&](int kk) {
        if (FL_K < ST_K && i > 1 && kk == FL_K) { asm volatile("s_waitcnt vmcnt(0)" ::: "memory"); flag(i - 2); }
        if (i > 0 && kk == ST_K) pstore(std::integral_constant<int, (i > 0 ? i - 1 : 0)>{});
        if (FL_K > ST_K && i > 0 && i < 8 && kk == FL_K) { asm volatile("s_waitcnt vmcnt(0) lgkmcnt(0)" ::: "memory"); flag(i - 1); }
      };
      hstage(18 + 2 * i, 0, [&](int k) {
        const int gap = k % 3 ? (k / 3) * 2 + k % 3 - 1 : -1;
        if (i == 0 && gap >= 0 && gap < 9) img(3 + gap);
        if (i == 0 && k == 23) { unsigned a = hdst + PARKOFF + l16; PIN(a); UNR for (int q = 0; q < 3; ++q) *(LAS u32x4*)(a + q * 1024) = hp_[7][q]; }
        if (i > 0 && gap >= 6 && gap < 12) pack((i - 1) & 1, gap - 6);
        xch(k);
      });
      if (i == 0) { VBAR(63); img(12); img(13); img(14); }
      hstage(19 + 2 * i, 16384, [&](int k) {
        const int gap = k % 3 ? (k / 3) * 2 + k % 3 - 1 : -1;
        if (i == 0 && gap >= 0 && gap < 9) img(15 + gap);
        xch(24 + k);
        if (i == 7 && gap == 0) { asm volatile("" : "+s"(hdst)); unsigned a = hdst + PARKOFF + l16; PIN(a); UNR for (int q = 0; q < 3; ++q) hp_[7][q] = *(volatile LAS u32x4*)(a + q * 1024); }
        if (i < 8 && gap >= 4) {
          const int j = gap - 4, q = j >> 2, e = j & 3;
          oc[(i + 1) & 1][e >> 1][q][(e & 1) * 2] = __uint_as_float(hp_[i & 7][q][e] << 16);
          oc[(i + 1) & 1][e >> 1][q][(e & 1) * 2 + 1] = __uint_as_float(hp_[i & 7][q][e] & 0xffff0000u);
          PIN(oc[(i + 1) & 1][e >> 1][q]);
        }
      });
      if (i < 8) LBAR();
    });
    UNR for (int k = 0; k < 9; ++k) oc[0][(k / 3 + 5) & 1][k % 3] = MFMA(wf[k / 3 + 5], of[4 + ((k / 3 + 5) >> 1)][k % 3], oc[0][(k / 3 + 5) & 1][k % 3]);
    TS(7);
    UNR for (int j = 0; j < 6; ++j) pack(0, j);
    pstore(std::integral_constant<int, 8>{});
    asm volatile("s_cmp_eq_u32 %0, 0\n\ts_waitcnt vmcnt(3) lgkmcnt(0)\n\ts_cbranch_scc0 1f\n\ts_waitcnt vmcnt(0)\n1:" ::"s"(oth1) : "memory", "scc");
    flag(7);
    unsigned tme;
    ld_word(tme, myline + 64);
    asm volatile("s_waitcnt vmcnt(0) lgkmcnt(0)" : "+v"(tme) :: "memory");
    flag(8);
    TS(8);
    PIN(l16);
    ln = l16 >> 4;
    spin((const unsigned*)myline + gw * 2 + (ln & 1), tme, 0);
    unsigned t0, t1;
    ld_word(t0, peer + 64); ld_word(t1, peer + 192);
    asm volatile("s_waitcnt vmcnt(0)" : "+v"(t0), "+v"(t1) :: "memory");
    t0 = __builtin_amdgcn_readfirstlane(t0); t1 = __builtin_amdgcn_readfirstlane(t1);
    if (__builtin_expect(t0 != tp0, 0)) st_flag(peer + hg * 4, t0 + 4);
    if (__builtin_expect(t1 != tp1, 0)) st_flag(peer + 128 + hg * 4, t1 + 3);
  }
  asm volatile("mha_code_end:" ::: "memory");
  TS(9);
  TSOUT(ws, blockIdx.x, 0, 10);
}

extern "C" void kernel_launch(void* const* d_in, const int* in_sizes, int n_in, void* d_out, int out_size, void* d_ws, size_t ws_size,
                              hipStream_t stream) {
  (void)in_sizes; (void)n_in; (void)out_size; (void)ws_size;
  (void)hipFuncSetAttribute((const void*)mha_kernel, hipFuncAttributeMaxDynamicSharedMemorySize, LDS_A);
  mha_kernel<<<256, 512, LDS_A, stream>>>((const char*)d_in[0], (const char*)d_in[1], (const char*)d_in[2], (const char*)d_in[3], (const char*)d_in[4],
                                          (const char*)d_in[5], (const char*)d_in[6], (const char*)d_in[7], (const char*)d_in[8], (const char*)d_in[9],
                                          (char*)d_ws, (char*)d_out);
}
```
